```python
import torch

def _softmax(x, axis=-1):
    return torch.softmax(x, dim=axis)
def _gelu(x):
    return torch.nn.functional.gelu(x, approximate="tanh")
def _silu(x):
    return torch.nn.functional.silu(x)
def _sigmoid(x):
    return torch.sigmoid(x)
def _layernorm(x, axis=-1, eps=1e-5):
    m = x.mean(dim=axis, keepdim=True)
    v = x.var(dim=axis, keepdim=True, unbiased=False)
    return (x - m) / torch.sqrt(v + eps)
def _scatter_add(tgt, idx, src, axis=0):
    return tgt.clone().index_add_(axis, idx, src)
def _segment_sum(src, seg, n):
    out = torch.zeros((n,) + src.shape[1:], dtype=src.dtype)
    return out.index_add_(0, seg, src)
def _pad(x, pw):
    flat = tuple(v for pair in reversed(pw) for v in pair)
    return torch.nn.functional.pad(x, flat)

kernel_name = 'standalone__mha_block_cached__decode'

def setup_inputs():

    _w = (lambda a: (lambda t: t.to(torch.bfloat16) if t.is_floating_point() else t)(torch.as_tensor(a)))
    B, Sq, D = 4, 1, 512
    H, Sk, Dh = 32, 4096, 128
    return {
        "x": _w((lambda *s: torch.randn(*s, dtype=torch.bfloat16) * 0.8)(B, Sq, D)),
        "k_cache": _w((lambda *s: torch.randn(*s, dtype=torch.bfloat16) * 0.8)(B, H, Sk, Dh)),
        "v_cache": _w((lambda *s: torch.randn(*s, dtype=torch.bfloat16) * 0.05)(B, H, Sk, Dh)),
        "wq": _w((lambda *s: torch.randn(*s, dtype=torch.bfloat16) * 0.8)(D, H * Dh) * (D ** -0.5)),
        "wo": _w((lambda *s: torch.randn(*s, dtype=torch.bfloat16) * 0.05)(H * Dh, D) * ((H * Dh) ** -0.5)),
    }

def reference(x, k_cache, v_cache, wq, wo):
    B, Sq, D = x.shape
    H, Dh = 32, 128
    q = (lambda x, *d: x.permute(*d))((x @ wq).reshape(B, Sq, H, Dh), 0, 2, 1, 3)
    scores = torch.einsum("bhqd,bhkd->bhqk", q, k_cache) * (Dh ** -0.5)
    p = _softmax(scores, axis=-1)
    o = torch.einsum("bhqk,bhkd->bhqd", p, v_cache)
    return (lambda x, *d: x.permute(*d))(o, 0, 2, 1, 3).reshape(B, Sq, H * Dh) @ wo
```

```cpp
#include <hip/hip_runtime.h>
#include <hip/hip_bf16.h>
#include <stdint.h>

#define KV_NT 1
#define DEPTH 4

constexpr int NB = 4, DM = 512, NH = 32, SK = 4096, DH = 128;
constexpr int NSPLIT = 2, NWG = NB * NH * NSPLIT;
constexpr int NW = 8, NTHR = NW * 64;
constexpr int KEYS_WG = SK / NSPLIT;
constexpr int TILE = 16;
constexpr int NT = KEYS_WG / (NW * TILE);
constexpr float C2 = 0.088388347648318440f * 1.4426950408889634f;

using bf16x2 = __attribute__((ext_vector_type(2))) __bf16;
using u32x4  = __attribute__((ext_vector_type(4))) unsigned;
using f32x4  = __attribute__((ext_vector_type(4))) float;
using f32x2  = __attribute__((ext_vector_type(2))) float;

__device__ __forceinline__ float dot2(unsigned a, unsigned b, float acc) {
  return __builtin_amdgcn_fdot2_f32_bf16(__builtin_bit_cast(bf16x2, a), __builtin_bit_cast(bf16x2, b), acc, false);
}
__device__ __forceinline__ unsigned cvtpk(float lo, float hi) {
  bf16x2 r = {(__bf16)lo, (__bf16)hi};
  return __builtin_bit_cast(unsigned, r);
}
template <int CTRL> __device__ __forceinline__ float dpp(float x) {
  return __builtin_bit_cast(float, __builtin_amdgcn_mov_dpp(__builtin_bit_cast(int, x), CTRL, 0xf, 0xf, true));
}
constexpr int XOR1 = 0xB1, XOR2 = 0x4E, XOR7 = 0x141, XOR8 = 0x128;
__device__ __forceinline__ float row_allsum(float x) {
  x += dpp<XOR1>(x); x += dpp<XOR2>(x); x += dpp<XOR7>(x); x += dpp<XOR8>(x); return x;
}
__device__ __forceinline__ float fold32(float a, float b) {
  auto t = __builtin_amdgcn_permlane32_swap(__float_as_uint(a), __float_as_uint(b), false, false);
  return __uint_as_float(t[0]) + __uint_as_float(t[1]);
}
__device__ __forceinline__ float fold16(float a, float b) {
  auto t = __builtin_amdgcn_permlane16_swap(__float_as_uint(a), __float_as_uint(b), false, false);
  return __uint_as_float(t[0]) + __uint_as_float(t[1]);
}
__device__ __forceinline__ float xrow_max(float x) {
  auto s = __builtin_amdgcn_permlane16_swap(__float_as_uint(x), __float_as_uint(x), false, false);
  x = fmaxf(__uint_as_float(s[0]), __uint_as_float(s[1]));
  auto t = __builtin_amdgcn_permlane32_swap(__float_as_uint(x), __float_as_uint(x), false, false);
  return fmaxf(__uint_as_float(t[0]), __uint_as_float(t[1]));
}
__device__ __forceinline__ u32x4 ldkv(const u32x4* p) {
  return __builtin_nontemporal_load(p);
}
__device__ __forceinline__ float bflo(unsigned u) { return __uint_as_float(u << 16); }
__device__ __forceinline__ float bfhi(unsigned u) { return __uint_as_float(u & 0xffff0000u); }

constexpr size_t WS_Y = 0, WS_ML = (size_t)NWG * DM * 4;

__global__ __launch_bounds__(NTHR, 2) void mha_main(const u32x4* __restrict__ x, const u32x4* __restrict__ kc,
                                                    const u32x4* __restrict__ vc, const u32x4* __restrict__ wq,
                                                    const u32x4* __restrict__ wo, float* __restrict__ ws) {
  __shared__ __attribute__((aligned(16))) float lds[NW * 128 + NW * 128 + NW * 2 + NW * 64];
  float* l_q = lds;
  float* l_a = lds + NW * 128;
  float* l_ml = lds + 2 * NW * 128;
  unsigned* l_o = reinterpret_cast<unsigned*>(lds + 2 * NW * 128 + NW * 2);

  const int bid = blockIdx.x;
  const int h = (bid & 7) * 4 + ((bid >> 3) & 3);
  const int bs = bid >> 5, b = bs >> 1, s = bs & 1;
  const int tid = threadIdx.x, lane = tid & 63, w = __builtin_amdgcn_readfirstlane(tid >> 6);
  const int r = lane >> 4, c = lane & 15;

  const u32x4* xp = x + (b * DM + 64 * w + 16 * r) / 8;
  u32x4 xv0 = xp[0], xv1 = xp[1];
  const u32x4* wqp = wq + (size_t)(64 * w + 16 * r) * (NH * DH / 8) + h * (DH / 8) + c;
  u32x4 wv[16];
#pragma unroll
  for (int i = 0; i < 16; ++i) wv[i] = wqp[(size_t)i * (NH * DH / 8)];

  const size_t kvoff = ((size_t)(b * NH + h) * SK + s * KEYS_WG + w * TILE) * (DH / 8) + lane;
  const u32x4* kp = kc + kvoff;
  const u32x4* vp = vc + kvoff;
  constexpr int TSTR = NW * TILE * (DH / 8);
  u32x4 Kr[DEPTH][4], Vr[DEPTH][4];
#pragma unroll
  for (int d = 0; d < DEPTH; ++d) {
#pragma unroll
    for (int t = 0; t < 4; ++t) Kr[d][t] = ldkv(kp + d * TSTR + t * 64);
#pragma unroll
    for (int t = 0; t < 4; ++t) Vr[d][t] = ldkv(vp + d * TSTR + t * 64);
  }
  __builtin_amdgcn_sched_barrier(0);

  unsigned qpk[4];
  {
    float qa[8];
#pragma unroll
    for (int k = 0; k < 8; ++k) qa[k] = 0.f;
#pragma unroll
    for (int j = 0; j < 8; ++j) {
      const unsigned xpair = j < 4 ? xv0[j] : xv1[j - 4];
#pragma unroll
      for (int k = 0; k < 4; ++k) {
        const unsigned lo = __builtin_amdgcn_perm(wv[2 * j + 1][k], wv[2 * j][k], 0x05040100u);
        const unsigned hi = __builtin_amdgcn_perm(wv[2 * j + 1][k], wv[2 * j][k], 0x07060302u);
        qa[2 * k] = dot2(lo, xpair, qa[2 * k]);
        qa[2 * k + 1] = dot2(hi, xpair, qa[2 * k + 1]);
      }
    }
    float u[4], t2[2];
#pragma unroll
    for (int j = 0; j < 4; ++j) u[j] = fold32(qa[j], qa[j + 4]);
#pragma unroll
    for (int j = 0; j < 2; ++j) t2[j] = fold16(u[j], u[j + 2]);
    *reinterpret_cast<f32x2*>(l_q + w * 128 + 8 * c + 2 * r) = f32x2{t2[0], t2[1]};
    __syncthreads();
    float q0 = 0.f, q1 = 0.f;
#pragma unroll
    for (int j = 0; j < NW; ++j) {
      const f32x2 p = *reinterpret_cast<const f32x2*>(l_q + j * 128 + 8 * c + 2 * r);
      q0 += p[0]; q1 += p[1];
    }
    const unsigned pk = cvtpk(q0, q1);
    auto s16 = __builtin_amdgcn_permlane16_swap(pk, pk, false, false);
    auto e = __builtin_amdgcn_permlane32_swap(s16[0], s16[0], false, false);
    auto o = __builtin_amdgcn_permlane32_swap(s16[1], s16[1], false, false);
    qpk[0] = e[0]; qpk[1] = o[0]; qpk[2] = e[1]; qpk[3] = o[1];
  }

  float m = -INFINITY, l = 0.f, acc[8];
#pragma unroll
  for (int k = 0; k < 8; ++k) acc[k] = 0.f;

  auto step = [&](u32x4 (&K)[4], u32x4 (&V)[4], int next, bool more) {
    float sc[4];
#pragma unroll
    for (int t = 0; t < 4; ++t) {
      float d = dot2(K[t][0], qpk[0], 0.f);
      d = dot2(K[t][1], qpk[1], d); d = dot2(K[t][2], qpk[2], d); d = dot2(K[t][3], qpk[3], d);
      sc[t] = d;
    }
    if (more) {
#pragma unroll
      for (int t = 0; t < 4; ++t) K[t] = ldkv(kp + (size_t)next * TSTR + t * 64);
    }
#pragma unroll
    for (int t = 0; t < 4; ++t) sc[t] = row_allsum(sc[t]) * C2;
    const float mn = fmaxf(fmaxf(m, fmaxf(sc[0], sc[1])), fmaxf(sc[2], sc[3]));
    const float al = __builtin_amdgcn_exp2f(m - mn);
    m = mn;
    float p[4];
#pragma unroll
    for (int t = 0; t < 4; ++t) p[t] = __builtin_amdgcn_exp2f(sc[t] - mn);
    l = fmaf(l, al, (p[0] + p[1]) + (p[2] + p[3]));
#pragma unroll
    for (int k = 0; k < 8; ++k) acc[k] *= al;
#pragma unroll
    for (int t = 0; t < 4; ++t) {
#pragma unroll
      for (int k = 0; k < 4; ++k) {
        acc[2 * k] = fmaf(p[t], bflo(V[t][k]), acc[2 * k]);
        acc[2 * k + 1] = fmaf(p[t], bfhi(V[t][k]), acc[2 * k + 1]);
      }
    }
    if (more) {
#pragma unroll
      for (int t = 0; t < 4; ++t) V[t] = ldkv(vp + (size_t)next * TSTR + t * 64);
    }
    __builtin_amdgcn_sched_barrier(0);
  };

  static_assert(NT % DEPTH == 0, "");
  for (int it = 0; it < NT - DEPTH; it += DEPTH) {
#pragma unroll
    for (int d = 0; d < DEPTH; ++d) step(Kr[d], Vr[d], it + d + DEPTH, true);
  }
  const int rg = lane >> 3, cg = lane & 7;
  const u32x4* wop = wo + (size_t)(h * DH + 16 * rg) * (DM / 8) + 8 * w + cg;
  u32x4 wov[16];
#pragma unroll
  for (int d = 0; d < DEPTH; ++d) {
    step(Kr[d], Vr[d], 0, false);
    if (d < 2) {
#pragma unroll
      for (int i = 0; i < 8; ++i) wov[8 * d + i] = wop[(size_t)(8 * d + i) * (DM / 8)];
      __builtin_amdgcn_sched_barrier(0);
    }
  }

  {
    const float mw = xrow_max(m);
    const float f = __builtin_amdgcn_exp2f(m - mw);
    l *= f;
#pragma unroll
    for (int k = 0; k < 8; ++k) acc[k] *= f;
    float u[4], t2[2];
#pragma unroll
    for (int j = 0; j < 4; ++j) u[j] = fold32(acc[j], acc[j + 4]);
#pragma unroll
    for (int j = 0; j < 2; ++j) t2[j] = fold16(u[j], u[j + 2]);
    l = fold32(l, l); l = fold16(l, l);
    *reinterpret_cast<f32x2*>(l_a + w * 128 + 8 * c + 2 * r) = f32x2{t2[0], t2[1]};
    if (lane == 0) *reinterpret_cast<f32x2*>(l_ml + 2 * w) = f32x2{mw, l};
  }
  unsigned wp[8][8];
#pragma unroll
  for (int j = 0; j < 8; ++j)
#pragma unroll
    for (int k = 0; k < 4; ++k) {
      wp[j][2 * k] = __builtin_amdgcn_perm(wov[2 * j + 1][k], wov[2 * j][k], 0x05040100u);
      wp[j][2 * k + 1] = __builtin_amdgcn_perm(wov[2 * j + 1][k], wov[2 * j][k], 0x07060302u);
    }
  __syncthreads();
  {
    f32x2 ml[NW], a[NW];
#pragma unroll
    for (int j = 0; j < NW; ++j) {
      ml[j] = *reinterpret_cast<const f32x2*>(l_ml + 2 * j);
      a[j] = *reinterpret_cast<const f32x2*>(l_a + j * 128 + 8 * c + 2 * r);
    }
    float M = ml[0][0];
#pragma unroll
    for (int j = 1; j < NW; ++j) M = fmaxf(M, ml[j][0]);
    float L = 0.f, o0 = 0.f, o1 = 0.f;
#pragma unroll
    for (int j = 0; j < NW; ++j) {
      const float wt = __builtin_amdgcn_exp2f(ml[j][0] - M);
      L = fmaf(wt, ml[j][1], L); o0 = fmaf(wt, a[j][0], o0); o1 = fmaf(wt, a[j][1], o1);
    }
    l_o[w * 64 + 4 * c + r] = cvtpk(o0, o1);
    __builtin_amdgcn_fence(__ATOMIC_RELEASE, "wavefront");
    __builtin_amdgcn_wave_barrier();
    const u32x4 oa = *reinterpret_cast<const u32x4*>(l_o + w * 64 + 8 * rg);
    const u32x4 ob = *reinterpret_cast<const u32x4*>(l_o + w * 64 + 8 * rg + 4);
    float y[8];
#pragma unroll
    for (int k = 0; k < 8; ++k) y[k] = 0.f;
#pragma unroll
    for (int j = 0; j < 8; ++j) {
      const unsigned op = j < 4 ? oa[j] : ob[j - 4];
#pragma unroll
      for (int k = 0; k < 8; ++k) y[k] = dot2(wp[j][k], op, y[k]);
    }
    float u[4], t2[2];
#pragma unroll
    for (int j = 0; j < 4; ++j) u[j] = fold32(y[j], y[j + 4]);
#pragma unroll
    for (int j = 0; j < 2; ++j) t2[j] = fold16(u[j], u[j + 2]);
    const float ya = t2[0] + dpp<XOR8>(t2[0]), yb = t2[1] + dpp<XOR8>(t2[1]);
    const float yo = (lane & 8) ? yb : ya;
    const int slot = (b * NH + h) * NSPLIT + s;
    ws[(size_t)slot * DM + 64 * w + 8 * cg + rg] = yo;
    if (tid == 0) *reinterpret_cast<f32x2*>(ws + WS_ML / 4 + 2 * slot) = f32x2{M, L};
  }
}

__global__ __launch_bounds__(64) void mha_combine(const float* __restrict__ ws, unsigned short* __restrict__ out) {
  const int b = blockIdx.x >> 5, cc = blockIdx.x & 31;
  const int lane = threadIdx.x, pg = lane >> 2, q = lane & 3;
  f32x4 y[2][2], ml[2];
#pragma unroll
  for (int i = 0; i < 2; ++i) {
    const int slot = (b * NH + pg + 16 * i) * NSPLIT;
    ml[i] = *reinterpret_cast<const f32x4*>(ws + WS_ML / 4 + 2 * slot);
#pragma unroll
    for (int s = 0; s < 2; ++s) y[i][s] = *reinterpret_cast<const f32x4*>(ws + (size_t)(slot + s) * DM + 16 * cc + 4 * q);
  }
  float o[4] = {0.f, 0.f, 0.f, 0.f};
#pragma unroll
  for (int i = 0; i < 2; ++i) {
    const float M = fmaxf(ml[i][0], ml[i][2]);
    const float w0 = __builtin_amdgcn_exp2f(ml[i][0] - M), w1 = __builtin_amdgcn_exp2f(ml[i][2] - M);
    const float inv = 1.f / fmaf(w0, ml[i][1], w1 * ml[i][3]);
    const float f0 = w0 * inv, f1 = w1 * inv;
#pragma unroll
    for (int k = 0; k < 4; ++k) o[k] += fmaf(f0, y[i][0][k], f1 * y[i][1][k]);
  }
  float u0 = fold32(o[0], o[2]), u1 = fold32(o[1], o[3]);
  float t = fold16(u0, u1);
  t += dpp<XOR8>(t);
  t += dpp<0x124>(t);
  const int r = lane >> 4;
  if ((lane & 12) == 0) {
    const __bf16 v = (__bf16)t;
    out[b * DM + 16 * cc + 4 * q + r] = __builtin_bit_cast(unsigned short, v);
  }
}

extern "C" void kernel_launch(void* const* d_in, const int* in_sizes, int n_in, void* d_out, int out_size, void* d_ws,
                              size_t ws_size, hipStream_t stream) {
  mha_main<<<NWG, NTHR, 0, stream>>>((const u32x4*)d_in[0], (const u32x4*)d_in[1], (const u32x4*)d_in[2],
                                     (const u32x4*)d_in[3], (const u32x4*)d_in[4], (float*)d_ws);
  mha_combine<<<NB * 32, 64, 0, stream>>>((const float*)d_ws, (unsigned short*)d_out);
}
```

```cpp
#include <hip/hip_runtime.h>
#include <hip/hip_bf16.h>
#include <stdint.h>

#define W_NT 1
#define DEPTH 2
#define PEEL 2
#define DRY 1
#define ASYM 1

constexpr int NB = 4, DM = 512, NH = 32, SK = 4096, DH = 128;
constexpr int NSPLIT = 2, NWG = NB * NH * NSPLIT;
constexpr int NW = 8, NTHR = NW * 64;
constexpr int KEYS_WG = SK / NSPLIT;
constexpr int TILE = 16;
constexpr int NT = KEYS_WG / (NW * TILE);
constexpr float C2 = 0.088388347648318440f * 1.4426950408889634f;

using bf16x2 = __attribute__((ext_vector_type(2))) __bf16;
using u32x4  = __attribute__((ext_vector_type(4))) unsigned;
using f32x4  = __attribute__((ext_vector_type(4))) float;
using f32x2  = __attribute__((ext_vector_type(2))) float;

__device__ __forceinline__ float dot2(unsigned a, unsigned b, float acc) {
  return __builtin_amdgcn_fdot2_f32_bf16(__builtin_bit_cast(bf16x2, a), __builtin_bit_cast(bf16x2, b), acc, false);
}
__device__ __forceinline__ unsigned cvtpk(float lo, float hi) {
  bf16x2 r = {(__bf16)lo, (__bf16)hi};
  return __builtin_bit_cast(unsigned, r);
}
template <int CTRL> __device__ __forceinline__ float dpp(float x) {
  return __builtin_bit_cast(float, __builtin_amdgcn_mov_dpp(__builtin_bit_cast(int, x), CTRL, 0xf, 0xf, true));
}
constexpr int XOR1 = 0xB1, XOR2 = 0x4E, XOR7 = 0x141, XOR8 = 0x128;
__device__ __forceinline__ float row_allsum(float x) {
  x += dpp<XOR1>(x); x += dpp<XOR2>(x); x += dpp<XOR7>(x); x += dpp<XOR8>(x); return x;
}
__device__ __forceinline__ float fold32(float a, float b) {
  auto t = __builtin_amdgcn_permlane32_swap(__float_as_uint(a), __float_as_uint(b), false, false);
  return __uint_as_float(t[0]) + __uint_as_float(t[1]);
}
__device__ __forceinline__ float fold16(float a, float b) {
  auto t = __builtin_amdgcn_permlane16_swap(__float_as_uint(a), __float_as_uint(b), false, false);
  return __uint_as_float(t[0]) + __uint_as_float(t[1]);
}
__device__ __forceinline__ float xrow_max(float x) {
  auto s = __builtin_amdgcn_permlane16_swap(__float_as_uint(x), __float_as_uint(x), false, false);
  x = fmaxf(__uint_as_float(s[0]), __uint_as_float(s[1]));
  auto t = __builtin_amdgcn_permlane32_swap(__float_as_uint(x), __float_as_uint(x), false, false);
  return fmaxf(__uint_as_float(t[0]), __uint_as_float(t[1]));
}
__device__ __forceinline__ u32x4 ldkv(const u32x4* p) { return __builtin_nontemporal_load(p); }
#define W_AUX 2
#define NOW 0
template <int WHICH> __device__ __forceinline__ u32x4 ldw_(__amdgpu_buffer_rsrc_t rs, int voff, int soff) {
  if (NOW & WHICH) { u32x4 r; asm volatile("" : "=v"(r)); return r; }
  return __builtin_amdgcn_raw_buffer_load_b128(rs, voff, soff, W_AUX);
}
__device__ __forceinline__ float bflo(unsigned u) { return __uint_as_float(u << 16); }
__device__ __forceinline__ float bfhi(unsigned u) { return __uint_as_float(u & 0xffff0000u); }
#define PIN4(a) asm volatile("" : "+v"((a)[0]), "+v"((a)[1]), "+v"((a)[2]), "+v"((a)[3]))
#define SB() __builtin_amdgcn_sched_barrier(0)

#define STAMP(i)

constexpr size_t WS_Y = 0, WS_ML = (size_t)NWG * DM * 4;

__global__ __launch_bounds__(NTHR, 2) void mha_main(const u32x4* __restrict__ x, const u32x4* __restrict__ kc,
                                                    const u32x4* __restrict__ vc, const u32x4* __restrict__ wq,
                                                    const u32x4* __restrict__ wo, float* __restrict__ ws) {
  __shared__ __attribute__((aligned(16))) float lds[NW * 128 + NW * 128 + NW * 2 + NW * 64];
  float* l_q = lds;
  float* l_a = lds + NW * 128;
  float* l_ml = lds + 2 * NW * 128;
  unsigned* l_o = reinterpret_cast<unsigned*>(lds + 2 * NW * 128 + NW * 2);

  const int bid = blockIdx.x;
  const int h = (bid & 7) * 4 + ((bid >> 3) & 3);
  const int bs = bid >> 5, b = bs >> 1, s = bs & 1;
  const int tid = threadIdx.x, lane = tid & 63, w = __builtin_amdgcn_readfirstlane(tid >> 6);
  const int r = lane >> 4, c = lane & 15;

  STAMP(0);
  const size_t kvoff = ((size_t)(b * NH + h) * SK + s * KEYS_WG + w * TILE) * (DH / 8) + lane;
  const u32x4* kp = kc + kvoff;
  const u32x4* vp = vc + kvoff;
  constexpr int TSTR = NW * TILE * (DH / 8);
  u32x4 Kr[DEPTH][4], Vr[DEPTH][4];
  const u32x4* xp = x + (b * DM + 64 * w + 16 * r) / 8;
  const __amdgpu_buffer_rsrc_t rq = __builtin_amdgcn_make_buffer_rsrc((void*)wq, 0, DM * NH * DH * 2, 0x00020000);
  const int wqv = (16 * r) * (NH * DH * 2) + 16 * c, wqs = 64 * w * (NH * DH * 2) + h * (DH * 2);
  u32x4 wv[16];
  Kr[0][0] = ldkv(kp);
  SB();
  u32x4 xv0 = xp[0], xv1 = xp[1];
  SB();
#pragma unroll
  for (int t = 0; t < 8; ++t) {
    if (t > 0 && t < 4) Kr[0][t] = ldkv(kp + t * 64);
    if (t >= 4) Vr[0][t - 4] = ldkv(vp + (t - 4) * 64);
    SB();
    wv[2 * t] = ldw_<1>(rq, wqv, wqs + (2 * t) * (NH * DH * 2));
    wv[2 * t + 1] = ldw_<1>(rq, wqv, wqs + (2 * t + 1) * (NH * DH * 2));
    SB();
  }

  unsigned qpk[4];
  {
    float qa[8];
#pragma unroll
    for (int k = 0; k < 8; ++k) qa[k] = 0.f;
#pragma unroll
    for (int j = 0; j < 8; ++j) {
      const unsigned xpair = j < 4 ? xv0[j] : xv1[j - 4];
#pragma unroll
      for (int k = 0; k < 4; ++k) {
        const unsigned lo = __builtin_amdgcn_perm(wv[2 * j + 1][k], wv[2 * j][k], 0x05040100u);
        const unsigned hi = __builtin_amdgcn_perm(wv[2 * j + 1][k], wv[2 * j][k], 0x07060302u);
        qa[2 * k] = dot2(lo, xpair, qa[2 * k]);
        qa[2 * k + 1] = dot2(hi, xpair, qa[2 * k + 1]);
      }
    }
    float u[4], t2[2];
#pragma unroll
    for (int j = 0; j < 4; ++j) u[j] = fold32(qa[j], qa[j + 4]);
#pragma unroll
    for (int j = 0; j < 2; ++j) t2[j] = fold16(u[j], u[j + 2]);
    *reinterpret_cast<f32x2*>(l_q + w * 128 + 8 * c + 2 * r) = f32x2{t2[0], t2[1]};
  }
  SB();
  asm volatile("s_waitcnt lgkmcnt(0)" ::: "memory");
  if (!ASYM || w >= NW / 2) __builtin_amdgcn_s_barrier();
  SB();
#pragma unroll
  for (int t = 0; t < 4; ++t) Kr[1][t] = ldkv(kp + TSTR + t * 64);
#pragma unroll
  for (int t = 0; t < 4; ++t) Vr[1][t] = ldkv(vp + TSTR + t * 64);
  SB();
  if (ASYM && w < NW / 2) __builtin_amdgcn_s_barrier();
  asm volatile("" ::: "memory");
  SB();
  {
    float q0 = 0.f, q1 = 0.f;
#pragma unroll
    for (int j = 0; j < NW; ++j) {
      const f32x2 p = *reinterpret_cast<const f32x2*>(l_q + j * 128 + 8 * c + 2 * r);
      q0 += p[0]; q1 += p[1];
    }
    const unsigned pk = cvtpk(q0, q1);
    auto s16 = __builtin_amdgcn_permlane16_swap(pk, pk, false, false);
    auto e = __builtin_amdgcn_permlane32_swap(s16[0], s16[0], false, false);
    auto o = __builtin_amdgcn_permlane32_swap(s16[1], s16[1], false, false);
    qpk[0] = e[0]; qpk[1] = o[0]; qpk[2] = e[1]; qpk[3] = o[1];
  }
  PIN4(qpk);
  STAMP(1);
  SB();
  const int rg = lane >> 3, cg = lane & 7;
  const __amdgpu_buffer_rsrc_t ro = __builtin_amdgcn_make_buffer_rsrc((void*)wo, 0, DM * NH * DH * 2, 0x00020000);
  const int wov_ = (16 * rg) * (DM * 2) + 16 * cg, wos = h * DH * (DM * 2) + 128 * w;
  u32x4 wov[16];
#pragma unroll
  for (int d = 2; d < DEPTH; ++d) {
#pragma unroll
    for (int t = 0; t < 4; ++t) Kr[d][t] = ldkv(kp + d * TSTR + t * 64);
#pragma unroll
    for (int t = 0; t < 4; ++t) Vr[d][t] = ldkv(vp + d * TSTR + t * 64);
  }
  SB();

  float m = -INFINITY, l = 0.f, acc[8];
#pragma unroll
  for (int k = 0; k < 8; ++k) acc[k] = 0.f;

  auto step = [&](u32x4 (&K)[4], u32x4 (&V)[4], const u32x4* kn, const u32x4* vn, bool more, int wo0, int won) {
    float sc[4];
    PIN4(K);
#pragma unroll
    for (int t = 0; t < 4; ++t) {
      float d = dot2(K[t][0], qpk[0], 0.f);
      d = dot2(K[t][1], qpk[1], d); d = dot2(K[t][2], qpk[2], d); d = dot2(K[t][3], qpk[3], d);
      sc[t] = d;
    }
    PIN4(sc);
    if (more) {
#pragma unroll
      for (int t = 0; t < 4; ++t) K[t] = ldkv(kn + t * 64);
    }
#pragma unroll
    for (int i = 0; i < won; ++i) wov[wo0 + i] = ldw_<2>(ro, wov_ + (wo0 + i) * (DM * 2), wos);
    SB();
#pragma unroll
    for (int t = 0; t < 4; ++t) sc[t] = row_allsum(sc[t]) * C2;
    const float mn = fmaxf(fmaxf(m, fmaxf(sc[0], sc[1])), fmaxf(sc[2], sc[3]));
    const float al = __builtin_amdgcn_exp2f(m - mn);
    m = mn;
    float p[4];
#pragma unroll
    for (int t = 0; t < 4; ++t) p[t] = __builtin_amdgcn_exp2f(sc[t] - mn);
    l = fmaf(l, al, (p[0] + p[1]) + (p[2] + p[3]));
#pragma unroll
    for (int k = 0; k < 8; ++k) acc[k] *= al;
    PIN4(p);
    PIN4(V);
#pragma unroll
    for (int t = 0; t < 4; ++t) {
#pragma unroll
      for (int k = 0; k < 4; ++k) {
        acc[2 * k] = fmaf(p[t], bflo(V[t][k]), acc[2 * k]);
        acc[2 * k + 1] = fmaf(p[t], bfhi(V[t][k]), acc[2 * k + 1]);
      }
    }
    PIN4(acc); PIN4(acc + 4);
    if (more) {
#pragma unroll
      for (int t = 0; t < 4; ++t) V[t] = ldkv(vn + t * 64);
    }
#pragma unroll
    for (int i = 0; i < won; ++i) wov[wo0 + won + i] = ldw_<2>(ro, wov_ + (wo0 + won + i) * (DM * 2), wos);
    SB();
  };

  static_assert((NT - DEPTH - PEEL) % DEPTH == 0 && 16 % (2 * PEEL) == 0, "");
#pragma unroll
  for (int i = 0; i < PEEL; ++i) step(Kr[i % DEPTH], Vr[i % DEPTH], kp + (size_t)(i + DEPTH) * TSTR, vp + (size_t)(i + DEPTH) * TSTR, true, i * (16 / PEEL), 8 / PEEL);
#pragma unroll 1
  for (int it = PEEL; it < NT - DEPTH; it += DEPTH) {
#pragma unroll
    for (int d = 0; d < DEPTH; ++d) step(Kr[(PEEL + d) % DEPTH], Vr[(PEEL + d) % DEPTH], kp + (size_t)(it + d + DEPTH) * TSTR, vp + (size_t)(it + d + DEPTH) * TSTR, true, 0, 0);
  }
  STAMP(2);
  unsigned wp[8][8];
#pragma unroll
  for (int j = 0; j < 8; ++j)
#pragma unroll
    for (int k = 0; k < 4; ++k) {
      wp[j][2 * k] = __builtin_amdgcn_perm(wov[2 * j + 1][k], wov[2 * j][k], 0x05040100u);
      wp[j][2 * k + 1] = __builtin_amdgcn_perm(wov[2 * j + 1][k], wov[2 * j][k], 0x07060302u);
    }
#pragma unroll
  for (int j = 0; j < 8; ++j) { PIN4(wp[j]); PIN4(wp[j] + 4); }
  SB();
#pragma unroll
  for (int d = 0; d < DEPTH; ++d) step(Kr[(NT - DEPTH + d) % DEPTH], Vr[(NT - DEPTH + d) % DEPTH], kp, vp, false, 0, 0);
  STAMP(3);

  {
    const float mw = xrow_max(m);
    const float f = __builtin_amdgcn_exp2f(m - mw);
    l *= f;
#pragma unroll
    for (int k = 0; k < 8; ++k) acc[k] *= f;
    float u[4], t2[2];
#pragma unroll
    for (int j = 0; j < 4; ++j) u[j] = fold32(acc[j], acc[j + 4]);
#pragma unroll
    for (int j = 0; j < 2; ++j) t2[j] = fold16(u[j], u[j + 2]);
    l = fold32(l, l); l = fold16(l, l);
    *reinterpret_cast<f32x2*>(l_a + w * 128 + 8 * c + 2 * r) = f32x2{t2[0], t2[1]};
    if (lane == 0) *reinterpret_cast<f32x2*>(l_ml + 2 * w) = f32x2{mw, l};
  }
  STAMP(4);
  int pass = __builtin_amdgcn_readfirstlane((DRY && w < NW / 2) ? 0 : 1);
  asm volatile("" : "+s"(pass));
#pragma unroll 1
  for (; pass < 2; ++pass) {
    if (pass) __syncthreads();
    STAMP(5);
    f32x2 ml[NW], a[NW];
#pragma unroll
    for (int j = 0; j < NW; ++j) {
      ml[j] = *reinterpret_cast<const f32x2*>(l_ml + 2 * j);
      a[j] = *reinterpret_cast<const f32x2*>(l_a + j * 128 + 8 * c + 2 * r);
    }
    float M = ml[0][0];
#pragma unroll
    for (int j = 1; j < NW; ++j) M = fmaxf(M, ml[j][0]);
    float L = 0.f, o0 = 0.f, o1 = 0.f;
#pragma unroll
    for (int j = 0; j < NW; ++j) {
      const float wt = __builtin_amdgcn_exp2f(ml[j][0] - M);
      L = fmaf(wt, ml[j][1], L); o0 = fmaf(wt, a[j][0], o0); o1 = fmaf(wt, a[j][1], o1);
    }
    l_o[w * 64 + 4 * c + r] = cvtpk(o0, o1);
    __builtin_amdgcn_fence(__ATOMIC_RELEASE, "wavefront");
    __builtin_amdgcn_wave_barrier();
    STAMP(6);
    const u32x4 oa = *reinterpret_cast<const u32x4*>(l_o + w * 64 + 8 * rg);
    const u32x4 ob = *reinterpret_cast<const u32x4*>(l_o + w * 64 + 8 * rg + 4);
    float y[8];
#pragma unroll
    for (int k = 0; k < 8; ++k) y[k] = 0.f;
#pragma unroll
    for (int j = 0; j < 8; ++j) {
      const unsigned op = j < 4 ? oa[j] : ob[j - 4];
#pragma unroll
      for (int k = 0; k < 8; ++k) y[k] = dot2(wp[j][k], op, y[k]);
    }
    float u[4], t2[2];
#pragma unroll
    for (int j = 0; j < 4; ++j) u[j] = fold32(y[j], y[j + 4]);
#pragma unroll
    for (int j = 0; j < 2; ++j) t2[j] = fold16(u[j], u[j + 2]);
    const float ya = t2[0] + dpp<XOR8>(t2[0]), yb = t2[1] + dpp<XOR8>(t2[1]);
    const float yo = (lane & 8) ? yb : ya;
    const int slot = (b * NH + h) * NSPLIT + s;
    if (pass) ws[(size_t)slot * DM + 64 * w + 8 * cg + rg] = yo;
    if (pass && tid == 0) *reinterpret_cast<f32x2*>(ws + WS_ML / 4 + 2 * slot) = f32x2{M, L};
  }
  STAMP(7);
}

__global__ __launch_bounds__(64) void mha_combine(const float* __restrict__ ws, unsigned short* __restrict__ out) {
  const int b = blockIdx.x >> 5, cc = blockIdx.x & 31;
  const int lane = threadIdx.x, pg = lane >> 2, q = lane & 3;
  f32x4 y[2][2], ml[2];
#pragma unroll
  for (int i = 0; i < 2; ++i) {
    const int slot = (b * NH + pg + 16 * i) * NSPLIT;
    ml[i] = *reinterpret_cast<const f32x4*>(ws + WS_ML / 4 + 2 * slot);
#pragma unroll
    for (int s = 0; s < 2; ++s) y[i][s] = *reinterpret_cast<const f32x4*>(ws + (size_t)(slot + s) * DM + 16 * cc + 4 * q);
  }
  float o[4] = {0.f, 0.f, 0.f, 0.f};
#pragma unroll
  for (int i = 0; i < 2; ++i) {
    const float M = fmaxf(ml[i][0], ml[i][2]);
    const float w0 = __builtin_amdgcn_exp2f(ml[i][0] - M), w1 = __builtin_amdgcn_exp2f(ml[i][2] - M);
    const float inv = 1.f / fmaf(w0, ml[i][1], w1 * ml[i][3]);
    const float f0 = w0 * inv, f1 = w1 * inv;
#pragma unroll
    for (int k = 0; k < 4; ++k) o[k] += fmaf(f0, y[i][0][k], f1 * y[i][1][k]);
  }
  float u0 = fold32(o[0], o[2]), u1 = fold32(o[1], o[3]);
  float t = fold16(u0, u1);
  t += dpp<XOR8>(t);
  t += dpp<0x124>(t);
  const int r = lane >> 4;
  if ((lane & 12) == 0) {
    const __bf16 v = (__bf16)t;
    out[b * DM + 16 * cc + 4 * q + r] = __builtin_bit_cast(unsigned short, v);
  }
}

extern "C" void kernel_launch(void* const* d_in, const int* in_sizes, int n_in, void* d_out, int out_size, void* d_ws,
                              size_t ws_size, hipStream_t stream) {
  mha_main<<<NWG, NTHR, 0, stream>>>((const u32x4*)d_in[0], (const u32x4*)d_in[1], (const u32x4*)d_in[2],
                                     (const u32x4*)d_in[3], (const u32x4*)d_in[4], (float*)d_ws);
  mha_combine<<<NB * 32, 64, 0, stream>>>((const float*)d_ws, (unsigned short*)d_out);
}
```

```cpp
#include <hip/hip_runtime.h>
#include <hip/hip_bf16.h>
#include <stdint.h>

#define DEPTH 4
#define IL 3
#define PEEL 4

constexpr int NB = 4, DM = 512, NH = 32, SK = 4096, DH = 128;
constexpr int NSPLIT = 2, NWG = NB * NH * NSPLIT;
constexpr int NW = 8, NTHR = NW * 64;
constexpr int KEYS_WG = SK / NSPLIT;
constexpr int TILE = 16;
constexpr int NT = KEYS_WG / (NW * TILE);
constexpr float C2 = 0.088388347648318440f * 1.4426950408889634f;
constexpr int NTP = 2, SC1 = 16;
constexpr int OOB = 0x40000000;

using bf16x2 = __attribute__((ext_vector_type(2))) __bf16;
using bf16x8 = __attribute__((ext_vector_type(8))) __bf16;
using u32x2  = __attribute__((ext_vector_type(2))) unsigned;
using u32x4  = __attribute__((ext_vector_type(4))) unsigned;
using f32x2  = __attribute__((ext_vector_type(2))) float;
using f32x4  = __attribute__((ext_vector_type(4))) float;

__device__ __forceinline__ float dot2(unsigned a, unsigned b, float acc) {
  return __builtin_amdgcn_fdot2_f32_bf16(__builtin_bit_cast(bf16x2, a), __builtin_bit_cast(bf16x2, b), acc, false);
}
__device__ __forceinline__ unsigned cvtpk(float lo, float hi) {
  bf16x2 r = {(__bf16)lo, (__bf16)hi};
  return __builtin_bit_cast(unsigned, r);
}
template <int CTRL> __device__ __forceinline__ float dpp(float x) {
  return __builtin_bit_cast(float, __builtin_amdgcn_mov_dpp(__builtin_bit_cast(int, x), CTRL, 0xf, 0xf, true));
}
constexpr int XOR1 = 0xB1, XOR2 = 0x4E, XOR7 = 0x141, XOR8 = 0x128, ROR4 = 0x124, BC0 = 0x150;
template <int L> __device__ __forceinline__ unsigned rowbc(unsigned x) {
  return (unsigned)__builtin_amdgcn_mov_dpp((int)x, BC0 + L, 0xf, 0xf, true);
}
__device__ __forceinline__ float fold32(float a, float b) {
  auto t = __builtin_amdgcn_permlane32_swap(__float_as_uint(a), __float_as_uint(b), false, false);
  return __uint_as_float(t[0]) + __uint_as_float(t[1]);
}
__device__ __forceinline__ float fold16(float a, float b) {
  auto t = __builtin_amdgcn_permlane16_swap(__float_as_uint(a), __float_as_uint(b), false, false);
  return __uint_as_float(t[0]) + __uint_as_float(t[1]);
}
__device__ __forceinline__ float xrow_max(float x) {
  auto s = __builtin_amdgcn_permlane16_swap(__float_as_uint(x), __float_as_uint(x), false, false);
  x = fmaxf(__uint_as_float(s[0]), __uint_as_float(s[1]));
  auto t = __builtin_amdgcn_permlane32_swap(__float_as_uint(x), __float_as_uint(x), false, false);
  return fmaxf(__uint_as_float(t[0]), __uint_as_float(t[1]));
}
__device__ __forceinline__ u32x4 ld16(__amdgpu_buffer_rsrc_t rs, int voff, int soff) {
  return __builtin_amdgcn_raw_buffer_load_b128(rs, voff, soff, NTP);
}
__device__ __forceinline__ f32x4 mfma(u32x4 a, u32x4 b, f32x4 c) {
  return __builtin_amdgcn_mfma_f32_16x16x32_bf16(__builtin_bit_cast(bf16x8, a), __builtin_bit_cast(bf16x8, b), c, 0, 0, 0);
}
__device__ __forceinline__ __amdgpu_buffer_rsrc_t rsrc(const void* p, int bytes) {
  return __builtin_amdgcn_make_buffer_rsrc(const_cast<void*>(p), 0, bytes, 0x00020000);
}
#define PIN4(a) asm volatile("" : "+v"((a)[0]), "+v"((a)[1]), "+v"((a)[2]), "+v"((a)[3]))
#define SB() __builtin_amdgcn_sched_barrier(0)

constexpr int WS_EP = 0, WS_ML = 4096, WS_Y = 16384, WS_END = WS_Y + NB * 64 * 64 * 64;
constexpr int LSTR = 516;

__global__ __launch_bounds__(NTHR, 2) void mha_decode(const u32x4* __restrict__ x, const u32x4* __restrict__ kc,
                                                      const u32x4* __restrict__ vc, const u32x4* __restrict__ wq,
                                                      const u32x4* __restrict__ wo, unsigned* __restrict__ ws,
                                                      unsigned short* __restrict__ out) {
  __shared__ __attribute__((aligned(16))) float lds[NW * LSTR + 2 * NW];
  float* l_q = lds;
  float* l_a = lds + 128;
  float* l_ml = lds + NW * LSTR;

  const int bid = blockIdx.x;
  const int h = (bid & 7) * 4 + ((bid >> 3) & 3);
  const int bs = bid >> 5, b = bs >> 1, s = bs & 1;
  const int tid = threadIdx.x, lane = tid & 63, w = __builtin_amdgcn_readfirstlane(tid >> 6);
  const int r = lane >> 4, c = lane & 15;
  unsigned tag = __builtin_nontemporal_load(ws + WS_EP / 4 + 64 * b) + 1u;

  const __amdgpu_buffer_rsrc_t rk = rsrc(kc, NB * NH * SK * DH * 2), rv = rsrc(vc, NB * NH * SK * DH * 2);
  const __amdgpu_buffer_rsrc_t rq = rsrc(wq, DM * NH * DH * 2);
  const int kv0 = ((b * NH + h) * SK + s * KEYS_WG + w * TILE) * (DH * 2);
  const int l16 = lane * 16;
  constexpr int TSTR = NW * TILE * DH * 2;
  const u32x4* xp = x + (b * DM + 64 * w + 16 * r) / 8;
  const int wqv = (16 * r) * (NH * DH * 2) + 16 * c, wqs = 64 * w * (NH * DH * 2) + h * (DH * 2);
  u32x4 Kr[DEPTH][4], Vr[DEPTH][4], wv[16], xv0, xv1;
#pragma unroll
  for (int n = 0; n < 8 * DEPTH; ++n) {
    const int d = n >> 3, t = n & 3;
    if (n & 4) Vr[d][t] = ld16(rv, l16 + t * 1024, kv0 + d * TSTR); else Kr[d][t] = ld16(rk, l16 + t * 1024, kv0 + d * TSTR);
    SB();
    if (n == 0) { xv0 = xp[0]; xv1 = xp[1]; SB(); }
    if (n < 8 * IL) {
#pragma unroll
      for (int i = n * 2 / IL; i < (n + 1) * 2 / IL; ++i) wv[i] = ld16(rq, wqv, wqs + i * (NH * DH * 2));
      SB();
    }
  }
  unsigned qpk[4];
  {
    float qa[8];
#pragma unroll
    for (int k = 0; k < 8; ++k) qa[k] = 0.f;
#pragma unroll
    for (int j = 0; j < 8; ++j) {
      const unsigned xpair = j < 4 ? xv0[j] : xv1[j - 4];
#pragma unroll
      for (int k = 0; k < 4; ++k) {
        const unsigned lo = __builtin_amdgcn_perm(wv[2 * j + 1][k], wv[2 * j][k], 0x05040100u);
        const unsigned hi = __builtin_amdgcn_perm(wv[2 * j + 1][k], wv[2 * j][k], 0x07060302u);
        qa[2 * k] = dot2(lo, xpair, qa[2 * k]);
        qa[2 * k + 1] = dot2(hi, xpair, qa[2 * k + 1]);
      }
    }
    float u[4], t2[2];
#pragma unroll
    for (int j = 0; j < 4; ++j) u[j] = fold32(qa[j], qa[j + 4]);
#pragma unroll
    for (int j = 0; j < 2; ++j) t2[j] = fold16(u[j], u[j + 2]);
    *reinterpret_cast<f32x2*>(l_q + w * LSTR + 2 * lane) = f32x2{t2[0], t2[1]};
    SB();
    asm volatile("s_waitcnt lgkmcnt(0)\n\ts_barrier" ::: "memory");
    SB();
    float q0 = 0.f, q1 = 0.f;
#pragma unroll
    for (int j = 0; j < NW; ++j) {
      const f32x2 p = *reinterpret_cast<const f32x2*>(l_q + j * LSTR + 2 * lane);
      q0 += p[0]; q1 += p[1];
    }
    const unsigned pk = cvtpk(q0, q1);
    auto s16 = __builtin_amdgcn_permlane16_swap(pk, pk, false, false);
    auto e = __builtin_amdgcn_permlane32_swap(s16[0], s16[0], false, false);
    auto o = __builtin_amdgcn_permlane32_swap(s16[1], s16[1], false, false);
    qpk[0] = e[0]; qpk[1] = o[0]; qpk[2] = e[1]; qpk[3] = o[1];
  }
  PIN4(qpk);
  SB();

  const __amdgpu_buffer_rsrc_t ro = rsrc(wo, DM * NH * DH * 2);
  const int wov_ = 32 * r * (DM * 2) + 8 * c, wos = h * DH * (DM * 2) + 128 * w;
#define WOROW(i) ((2 * ((i) >> 3) + (((i) >> 2) & 1) + 16 * (((i) >> 1) & 1) + 8 * ((i) & 1)) * (DM * 2))
#define LDWO(i) wov[i] = __builtin_amdgcn_raw_buffer_load_b64(ro, wov_, wos + WOROW(i), NTP)
  u32x2 wov[32];

  float m = -3.0e38f, l = 0.f;
  f32x4 acc = {0.f, 0.f, 0.f, 0.f};
  const unsigned hm = (c & 1) ? 0xffff0000u : 0x0000ffffu;
  const unsigned mk0 = (c >> 1) == 0 ? hm : 0u, mk1 = (c >> 1) == 1 ? hm : 0u, mk2 = (c >> 1) == 2 ? hm : 0u, mk3 = (c >> 1) == 3 ? hm : 0u;
  const bool b3 = c & 8, b2 = c & 4;

  auto step = [&](u32x4 (&K)[4], u32x4 (&V)[4], int noff, bool more, int wo0, int won) {
    float sc[4];
    PIN4(K);
#pragma unroll
    for (int t = 0; t < 4; ++t) {
      float d = dot2(K[t][0], qpk[0], 0.f);
      d = dot2(K[t][1], qpk[1], d); d = dot2(K[t][2], qpk[2], d); d = dot2(K[t][3], qpk[3], d);
      sc[t] = d;
    }
    PIN4(sc);
    if (more) {
#pragma unroll
      for (int t = 0; t < 4; ++t) K[t] = ld16(rk, l16 + t * 1024, noff);
    }
#pragma unroll
    for (int i = 0; i < won; ++i) LDWO(wo0 + i);
    SB();
    const float a0 = sc[0] + dpp<XOR8>(sc[0]), a1 = sc[1] + dpp<XOR8>(sc[1]), a2 = sc[2] + dpp<XOR8>(sc[2]), a3 = sc[3] + dpp<XOR8>(sc[3]);
    const float u0 = b3 ? a1 : a0, u1 = b3 ? a3 : a2;
    const float e0 = u0 + dpp<XOR7>(u0), e1 = u1 + dpp<XOR7>(u1);
    float v = b2 ? e1 : e0;
    v += dpp<XOR1>(v); v += dpp<XOR2>(v);
    float d = fmaf(v, C2, -m);
    if (__builtin_amdgcn_ballot_w64(d > 0.f) != 0ull) {
      float mx = v * C2;
      mx = fmaxf(mx, dpp<ROR4>(mx)); mx = fmaxf(mx, dpp<XOR8>(mx)); mx = xrow_max(mx);
      const float mn = fmaxf(m, mx), al = __builtin_amdgcn_exp2f(m - mn);
      acc *= al; l *= al; m = mn;
      d = fmaf(v, C2, -m);
    }
    const float p = __builtin_amdgcn_exp2f(d);
    l += p;
    const unsigned pk = cvtpk(p, p);
    PIN4(V);
#define PV(t, L) { const unsigned bc = rowbc<L>(pk); acc = mfma(V[t], u32x4{bc & mk0, bc & mk1, bc & mk2, bc & mk3}, acc); }
    PV(0, 0) PV(1, 8) PV(2, 4) PV(3, 12)
#undef PV
    SB();
    if (more) {
#pragma unroll
      for (int t = 0; t < 4; ++t) V[t] = ld16(rv, l16 + t * 1024, noff);
    }
#pragma unroll
    for (int i = 0; i < won; ++i) LDWO(wo0 + won + i);
    SB();
  };

  static_assert((NT - DEPTH - PEEL) % DEPTH == 0 && 16 % PEEL == 0 && IL <= DEPTH, "");
#pragma unroll
  for (int i = 0; i < PEEL; ++i) step(Kr[i % DEPTH], Vr[i % DEPTH], kv0 + (i + DEPTH) * TSTR, true, i * (32 / PEEL), 16 / PEEL);
#pragma unroll 1
  for (int it = PEEL; it < NT - DEPTH; it += DEPTH) {
#pragma unroll
    for (int d = 0; d < DEPTH; ++d) step(Kr[(PEEL + d) % DEPTH], Vr[(PEEL + d) % DEPTH], kv0 + (it + d + DEPTH) * TSTR, true, 0, 0);
  }
  u32x4 wb[4][4];
#pragma unroll
  for (int ks = 0; ks < 4; ++ks) {
#pragma unroll
    for (int t = 0; t < 4; ++t) {
      const u32x2 lo = wov[8 * ks + 2 * t], hi = wov[8 * ks + 2 * t + 1];
      wb[ks][0][t] = __builtin_amdgcn_perm(hi[0], lo[0], 0x05040100u);
      wb[ks][1][t] = __builtin_amdgcn_perm(hi[0], lo[0], 0x07060302u);
      wb[ks][2][t] = __builtin_amdgcn_perm(hi[1], lo[1], 0x05040100u);
      wb[ks][3][t] = __builtin_amdgcn_perm(hi[1], lo[1], 0x07060302u);
    }
    PIN4(wb[ks]);
  }
  const __amdgpu_buffer_rsrc_t rw = rsrc(ws, WS_END);
  const int prod = s * NH + h;
  int yv = r < 2 ? (c >> 1) * 4096 + (2 * (c & 1) + r) * 16 : OOB;
  int ys = WS_Y + ((b * 64 + 8 * w) * 64 + prod) * 64, mls = WS_ML + (b * 64 + prod) * 16;
  int la_w = w * LSTR + 4 * (8 * r + c), la_r = 2 * lane, lm_r = 2 * (lane & 7);
  asm volatile("" : "+v"(yv), "+v"(la_w), "+v"(la_r), "+v"(lm_r));
  asm volatile("" : "+s"(ys), "+s"(mls), "+s"(tag));
  SB();
#pragma unroll
  for (int d = 0; d < DEPTH; ++d) step(Kr[(NT - DEPTH + d) % DEPTH], Vr[(NT - DEPTH + d) % DEPTH], 0, false, 0, 0);

  l += dpp<ROR4>(l); l += dpp<XOR8>(l);
  l = fold32(l, l); l = fold16(l, l);
  if (c < 8) *reinterpret_cast<f32x4*>(l_a + la_w) = acc;
  if (lane == 0) *reinterpret_cast<f32x2*>(l_ml + 2 * w) = f32x2{m, l};
  __syncthreads();
  {
    const f32x2 ml = *reinterpret_cast<const f32x2*>(l_ml + lm_r);
    f32x2 a[NW];
#pragma unroll
    for (int j = 0; j < NW; ++j) a[j] = *reinterpret_cast<const f32x2*>(l_a + j * LSTR + la_r);
    float M = ml[0];
    M = fmaxf(M, dpp<XOR1>(M)); M = fmaxf(M, dpp<XOR2>(M)); M = fmaxf(M, dpp<XOR7>(M));
    const float wt = __builtin_amdgcn_exp2f(ml[0] - M);
    float o0 = dpp<BC0>(wt) * a[0][0], o1 = dpp<BC0>(wt) * a[0][1], p0 = dpp<BC0 + 1>(wt) * a[1][0], p1 = dpp<BC0 + 1>(wt) * a[1][1];
#define MG(j) o0 = fmaf(dpp<BC0 + j>(wt), a[j][0], o0); o1 = fmaf(dpp<BC0 + j>(wt), a[j][1], o1); \
              p0 = fmaf(dpp<BC0 + j + 1>(wt), a[j + 1][0], p0); p1 = fmaf(dpp<BC0 + j + 1>(wt), a[j + 1][1], p1);
    MG(2) MG(4) MG(6)
#undef MG
    const unsigned opk = cvtpk(o0 + p0, o1 + p1);
    f32x4 y[4];
#define OP(ks) { const u32x4 A = {rowbc<4 * ks>(opk), rowbc<4 * ks + 1>(opk), rowbc<4 * ks + 2>(opk), rowbc<4 * ks + 3>(opk)}; \
      _Pragma("unroll") for (int e = 0; e < 4; ++e) y[e] = mfma(A, wb[ks][e], ks ? y[e] : f32x4{0.f, 0.f, 0.f, 0.f}); }
    OP(0) OP(1) OP(2) OP(3)
#undef OP
    if (w == 1) {
      float L = wt * ml[1];
      L += dpp<XOR1>(L); L += dpp<XOR2>(L); L += dpp<XOR7>(L);
      __builtin_amdgcn_raw_buffer_store_b128(u32x4{__float_as_uint(M), tag, __float_as_uint(L), tag}, rw, lane ? OOB : 0, mls, SC1);
      asm volatile("s_nop 1");
    }
    const float ya = r ? y[2][0] : y[0][0], yb = r ? y[3][0] : y[1][0];
    __builtin_amdgcn_raw_buffer_store_b128(u32x4{__float_as_uint(ya), tag, __float_as_uint(yb), tag}, rw, yv, ys, SC1);
    asm volatile("s_nop 1");
  }
  if (w == 0) {
    u32x4 Yv[4], Mv[4];
    bool ok[4];
#pragma unroll
    for (int k = 0; k < 4; ++k) { ok[k] = false; Yv[k] = u32x4{0, 0, 0, 0}; Mv[k] = u32x4{0, 0, 0, 0}; }
    const int ybase = WS_Y + (b * 64 + prod) * 4096, mbase = WS_ML + b * 1024;
    bool done = false;
#pragma unroll 1
    for (int spin = 0; spin < (1 << 18); ++spin) {
      int yo = l16, mo = (lane >> 2) * 16;
      asm volatile("" : "+v"(yo), "+v"(mo) :: "memory");
      u32x4 yn[4], mn[4];
#pragma unroll
      for (int k = 0; k < 4; ++k) {
        yn[k] = __builtin_amdgcn_raw_buffer_load_b128(rw, ok[k] ? OOB : yo + k * 1024, ybase, SC1);
        mn[k] = __builtin_amdgcn_raw_buffer_load_b128(rw, ok[k] ? OOB : mo + k * 256, mbase, SC1);
      }
      bool all = true;
#pragma unroll
      for (int k = 0; k < 4; ++k) {
        const bool now = yn[k][1] == tag && yn[k][3] == tag && mn[k][1] == tag && mn[k][3] == tag;
        if (!ok[k]) { Yv[k] = yn[k]; Mv[k] = mn[k]; }
        ok[k] = ok[k] || now;
        all = all && ok[k];
      }
      if (__all(all)) { done = true; break; }
    }
    float o0 = 0.f, o1 = 0.f;
#pragma unroll
    for (int i = 0; i < 2; ++i) {
      const float m0 = __uint_as_float(Mv[i][0]), l0 = __uint_as_float(Mv[i][2]), m1 = __uint_as_float(Mv[i + 2][0]), l1 = __uint_as_float(Mv[i + 2][2]);
      const float M = fmaxf(m0, m1);
      const float w0 = __builtin_amdgcn_exp2f(m0 - M), w1 = __builtin_amdgcn_exp2f(m1 - M);
      const float inv = __builtin_amdgcn_rcpf(fmaf(w0, l0, w1 * l1));
      const float f0 = w0 * inv, f1 = w1 * inv;
      o0 += fmaf(f0, __uint_as_float(Yv[i][0]), f1 * __uint_as_float(Yv[i + 2][0]));
      o1 += fmaf(f0, __uint_as_float(Yv[i][2]), f1 * __uint_as_float(Yv[i + 2][2]));
    }
    float t = fold32(o0, o1);
    t = fold16(t, t);
    t += dpp<XOR8>(t);
    t += dpp<ROR4>(t);
    if (!done) t = __builtin_nanf("");
    if ((lane & 0x1c) == 0) {
      const __bf16 v = (__bf16)t;
      out[b * DM + 8 * prod + 2 * (lane & 3) + (lane >> 5)] = __builtin_bit_cast(unsigned short, v);
    }
    if (prod == 0 && lane == 0 && done) ws[WS_EP / 4 + 64 * b] = tag;
  }
}

extern "C" void kernel_launch(void* const* d_in, const int* in_sizes, int n_in, void* d_out, int out_size, void* d_ws,
                              size_t ws_size, hipStream_t stream) {
  mha_decode<<<NWG, NTHR, 0, stream>>>((const u32x4*)d_in[0], (const u32x4*)d_in[1], (const u32x4*)d_in[2],
                                       (const u32x4*)d_in[3], (const u32x4*)d_in[4], (unsigned*)d_ws, (unsigned short*)d_out);
}
```

```cpp
#include <hip/hip_runtime.h>
#include <hip/hip_bf16.h>
#include <stdint.h>

constexpr int RING = 6;
constexpr int ILQ = 4;
constexpr int WOPS = 4;
constexpr int MLSTEP = 22;

constexpr int NB = 4, DM = 512, NH = 32, SK = 4096, DH = 128;
constexpr int NSPLIT = 2, NWG = NB * NH * NSPLIT;
constexpr int NW = 8, NTHR = NW * 64;
constexpr int KEYS_WG = SK / NSPLIT;
constexpr int TILE = 16;
constexpr int NT = KEYS_WG / (NW * TILE);
constexpr float C2 = 0.088388347648318440f * 1.4426950408889634f;
constexpr int NTP = 2, SC1 = 16;
constexpr int OOB = 0x40000000;

using bf16x2 = __attribute__((ext_vector_type(2))) __bf16;
using bf16x8 = __attribute__((ext_vector_type(8))) __bf16;
using u32x2  = __attribute__((ext_vector_type(2))) unsigned;
using u32x4  = __attribute__((ext_vector_type(4))) unsigned;
using f32x2  = __attribute__((ext_vector_type(2))) float;
using f32x4  = __attribute__((ext_vector_type(4))) float;

__device__ __forceinline__ float dot2(unsigned a, unsigned b, float acc) {
  return __builtin_amdgcn_fdot2_f32_bf16(__builtin_bit_cast(bf16x2, a), __builtin_bit_cast(bf16x2, b), acc, false);
}
__device__ __forceinline__ unsigned cvtpk(float lo, float hi) {
  bf16x2 r = {(__bf16)lo, (__bf16)hi};
  return __builtin_bit_cast(unsigned, r);
}
template <int CTRL> __device__ __forceinline__ float dpp(float x) {
  return __builtin_bit_cast(float, __builtin_amdgcn_mov_dpp(__builtin_bit_cast(int, x), CTRL, 0xf, 0xf, true));
}
constexpr int XOR1 = 0xB1, XOR2 = 0x4E, XOR7 = 0x141, XOR8 = 0x128, ROR4 = 0x124, BC0 = 0x150;
template <int L> __device__ __forceinline__ unsigned rowbc(unsigned x) {
  return (unsigned)__builtin_amdgcn_mov_dpp((int)x, BC0 + L, 0xf, 0xf, true);
}
__device__ __forceinline__ float fold32(float a, float b) {
  auto t = __builtin_amdgcn_permlane32_swap(__float_as_uint(a), __float_as_uint(b), false, false);
  return __uint_as_float(t[0]) + __uint_as_float(t[1]);
}
__device__ __forceinline__ float fold16(float a, float b) {
  auto t = __builtin_amdgcn_permlane16_swap(__float_as_uint(a), __float_as_uint(b), false, false);
  return __uint_as_float(t[0]) + __uint_as_float(t[1]);
}
__device__ __forceinline__ float xrow_max(float x) {
  auto s = __builtin_amdgcn_permlane16_swap(__float_as_uint(x), __float_as_uint(x), false, false);
  x = fmaxf(__uint_as_float(s[0]), __uint_as_float(s[1]));
  auto t = __builtin_amdgcn_permlane32_swap(__float_as_uint(x), __float_as_uint(x), false, false);
  return fmaxf(__uint_as_float(t[0]), __uint_as_float(t[1]));
}
__device__ __forceinline__ u32x4 ld16(__amdgpu_buffer_rsrc_t rs, int voff, int soff) {
  return __builtin_amdgcn_raw_buffer_load_b128(rs, voff, soff, NTP);
}
__device__ __forceinline__ f32x4 mfma(u32x4 a, u32x4 b, f32x4 c) {
  return __builtin_amdgcn_mfma_f32_16x16x32_bf16(__builtin_bit_cast(bf16x8, a), __builtin_bit_cast(bf16x8, b), c, 0, 0, 0);
}
__device__ __forceinline__ __amdgpu_buffer_rsrc_t rsrc(const void* p, int bytes) {
  return __builtin_amdgcn_make_buffer_rsrc(const_cast<void*>(p), 0, bytes, 0x00020000);
}
#define PIN4(a) asm volatile("" : "+v"((a)[0]), "+v"((a)[1]), "+v"((a)[2]), "+v"((a)[3]))
#define SB() __builtin_amdgcn_sched_barrier(0)

constexpr int WS_EP = 0, WS_ML = 4096, WS_Y = WS_ML + NB * 64 * 128, WS_END = WS_Y + NB * 64 * 64 * 64;
constexpr int LSTR = 516;

__global__ __launch_bounds__(NTHR, 2) void mha_decode(const u32x4* __restrict__ x, const u32x4* __restrict__ kc,
                                                      const u32x4* __restrict__ vc, const u32x4* __restrict__ wq,
                                                      const u32x4* __restrict__ wo, unsigned* __restrict__ ws,
                                                      unsigned short* __restrict__ out) {
  __shared__ __attribute__((aligned(16))) float lds[NW * LSTR + 2 * NW];
  float* l_q = lds;
  float* l_a = lds + 128;
  float* l_m = lds + NW * LSTR;
  float* l_l = l_m + NW;

  const int bid = blockIdx.x;
  const int h = ((bid & 3) | ((bid & 4) << 1)) ^ ((bid & 8) ? 7 : 0) ^ ((bid & 16) ? 25 : 0);
  const int bs = bid >> 5, b = bs >> 1, s = bs & 1;
  const int tid = threadIdx.x, lane = tid & 63, w = __builtin_amdgcn_readfirstlane(tid >> 6);
  const int r = lane >> 4, c = lane & 15;
  int kv0 = ((b * NH + h) * SK + s * KEYS_WG + (w >> 2) * 64 + (w & 3) * 4) * (DH * 2);
  int l16 = lane * 16;
  asm volatile("" : "+s"(kv0));
  asm volatile("" : "+v"(l16));
  SB();

  const __amdgpu_buffer_rsrc_t rk = rsrc(kc, NB * NH * SK * DH * 2), rv = rsrc(vc, NB * NH * SK * DH * 2);
  const __amdgpu_buffer_rsrc_t rq = rsrc(wq, DM * NH * DH * 2);
  constexpr int TSTR = NW * TILE * DH * 2;
  u32x4 R[RING][4];
#define LDT(g, t) R[(g) % RING][t] = ld16((g) < NT ? rk : rv, l16, kv0 + ((g) % NT) * TSTR + (t) * 4096)
#define BAR() asm volatile("s_waitcnt lgkmcnt(0)\n\ts_barrier" ::: "memory")

  const u32x4* xp = x + (b * DM + 64 * w + 16 * r) / 8;
  const int wqv = (16 * r) * (NH * DH * 2) + 16 * c, wqs = 64 * w * (NH * DH * 2) + h * (DH * 2);
  u32x4 wv[16], xv0, xv1;
  unsigned epoch;
  const __amdgpu_buffer_rsrc_t rw = rsrc(ws, WS_END);
#pragma unroll
  for (int n = 0; n < 4 * RING; ++n) {
    LDT(n >> 2, n & 3);
    SB();
    if (n == 0) { xv0 = xp[0]; xv1 = xp[1]; epoch = __builtin_amdgcn_raw_buffer_load_b32(rw, 0, WS_EP + 256 * b, SC1); SB(); }
    if (n < 4 * ILQ) {
#pragma unroll
      for (int i = n * 4 / ILQ; i < (n + 1) * 4 / ILQ; ++i) wv[i] = ld16(rq, wqv, wqs + i * (NH * DH * 2));
      SB();
    }
  }
  unsigned qpk[4];
  {
    float qa[8];
#pragma unroll
    for (int k = 0; k < 8; ++k) qa[k] = 0.f;
#pragma unroll
    for (int j = 0; j < 8; ++j) {
      const unsigned xpair = j < 4 ? xv0[j] : xv1[j - 4];
#pragma unroll
      for (int k = 0; k < 4; ++k) {
        const unsigned lo = __builtin_amdgcn_perm(wv[2 * j + 1][k], wv[2 * j][k], 0x05040100u);
        const unsigned hi = __builtin_amdgcn_perm(wv[2 * j + 1][k], wv[2 * j][k], 0x07060302u);
        qa[2 * k] = dot2(lo, xpair, qa[2 * k]);
        qa[2 * k + 1] = dot2(hi, xpair, qa[2 * k + 1]);
      }
    }
    float u[4], t2[2];
#pragma unroll
    for (int j = 0; j < 4; ++j) u[j] = fold32(qa[j], qa[j + 4]);
#pragma unroll
    for (int j = 0; j < 2; ++j) t2[j] = fold16(u[j], u[j + 2]);
    *reinterpret_cast<f32x2*>(l_q + w * LSTR + 2 * lane) = f32x2{t2[0], t2[1]};
    SB();
    BAR();
    SB();
    float q0 = 0.f, q1 = 0.f;
#pragma unroll
    for (int j = 0; j < NW; ++j) {
      const f32x2 p = *reinterpret_cast<const f32x2*>(l_q + j * LSTR + 2 * lane);
      q0 += p[0]; q1 += p[1];
    }
    const unsigned pk = cvtpk(q0, q1);
    auto s16 = __builtin_amdgcn_permlane16_swap(pk, pk, false, false);
    auto e = __builtin_amdgcn_permlane32_swap(s16[0], s16[0], false, false);
    auto o = __builtin_amdgcn_permlane32_swap(s16[1], s16[1], false, false);
    qpk[0] = e[0]; qpk[1] = o[0]; qpk[2] = e[1]; qpk[3] = o[1];
  }
  PIN4(qpk);
  unsigned tag = __builtin_amdgcn_readfirstlane(epoch) + 1u;
  asm volatile("" : "+s"(tag));
  SB();

  const __amdgpu_buffer_rsrc_t ro = rsrc(wo, DM * NH * DH * 2);
  const int wov_ = 32 * r * (DM * 2) + 8 * c, wos = h * DH * (DM * 2) + 128 * w;
#define WOROW(i) ((2 * ((i) >> 3) + (((i) >> 2) & 1) + 16 * (((i) >> 1) & 1) + 8 * ((i) & 1)) * (DM * 2))
  u32x2 wov[32];
  auto refill = [&](int g) {
    SB();
    BAR();
    SB();
    if (g + RING < 2 * NT) {
#pragma unroll
      for (int t = 0; t < 4; ++t) LDT(g + RING, t);
    }
#pragma unroll
    for (int i = g * WOPS; i < (g + 1) * WOPS; ++i)
      if (i < 32) wov[i] = __builtin_amdgcn_raw_buffer_load_b64(ro, wov_, wos + WOROW(i), NTP);
    SB();
  };

  const bool b3 = c & 8, b2 = c & 4;
  float sv[NT];
  float mx = -3.0e38f;
#pragma unroll
  for (int g = 0; g < NT; ++g) {
    u32x4 (&K)[4] = R[g % RING];
    float sc[4];
    PIN4(K);
#pragma unroll
    for (int t = 0; t < 4; ++t) {
      float d = dot2(K[t][0], qpk[0], 0.f);
      d = dot2(K[t][1], qpk[1], d); d = dot2(K[t][2], qpk[2], d); d = dot2(K[t][3], qpk[3], d);
      sc[t] = d;
    }
    PIN4(sc);
    if (g + 1 < NT) refill(g);
    const float a0 = sc[0] + dpp<XOR8>(sc[0]), a1 = sc[1] + dpp<XOR8>(sc[1]), a2 = sc[2] + dpp<XOR8>(sc[2]), a3 = sc[3] + dpp<XOR8>(sc[3]);
    const float u0 = b3 ? a1 : a0, u1 = b3 ? a3 : a2;
    const float e0 = u0 + dpp<XOR7>(u0), e1 = u1 + dpp<XOR7>(u1);
    float v = b2 ? e1 : e0;
    v += dpp<XOR1>(v); v += dpp<XOR2>(v);
    sv[g] = v * C2;
    mx = fmaxf(mx, sv[g]);
    asm volatile("" : "+v"(sv[g]), "+v"(mx));
  }
  mx = fmaxf(mx, dpp<ROR4>(mx)); mx = fmaxf(mx, dpp<XOR8>(mx)); mx = xrow_max(mx);
  if (lane == 0) l_m[w] = mx;
  refill(NT - 1);
  float M = l_m[lane & 7];
  M = fmaxf(M, dpp<XOR1>(M)); M = fmaxf(M, dpp<XOR2>(M)); M = fmaxf(M, dpp<XOR7>(M));
  unsigned pk[NT];
  {
    float l = 0.f;
#pragma unroll
    for (int g = 0; g < NT; ++g) { const float p = __builtin_amdgcn_exp2f(sv[g] - M); l += p; pk[g] = cvtpk(p, p); }
    l += dpp<ROR4>(l); l += dpp<XOR8>(l);
    l = fold32(l, l); l = fold16(l, l);
    if (lane == 0) l_l[w] = l;
  }
  PIN4(pk); PIN4(pk + 4); PIN4(pk + 8); PIN4(pk + 12);

  const int prod = s * NH + h;
  f32x4 acc = {0.f, 0.f, 0.f, 0.f};
  const unsigned hm = (c & 1) ? 0xffff0000u : 0x0000ffffu;
  const unsigned mk0 = (c >> 1) == 0 ? hm : 0u, mk1 = (c >> 1) == 1 ? hm : 0u, mk2 = (c >> 1) == 2 ? hm : 0u, mk3 = (c >> 1) == 3 ? hm : 0u;
  float L = 0.f, f = 0.f;
  bool have = false;
  auto share = [&](u32x4 u) {
    const float M2 = __uint_as_float(u[0]), L2 = __uint_as_float(u[2]);
    const float Mg = fmaxf(M, M2), w0 = __builtin_amdgcn_exp2f(M - Mg), w1 = __builtin_amdgcn_exp2f(M2 - Mg);
    f = w0 * __builtin_amdgcn_rcpf(fmaf(w0, L, w1 * L2));
    have = __builtin_amdgcn_readfirstlane(u[1]) == tag && __builtin_amdgcn_readfirstlane(u[3]) == tag;
  };
  u32x4 pml, wb[4][4];
  int yv, ys, yb, la_w, la_r, oo, eo;
#pragma unroll
  for (int g = NT; g < 2 * NT; ++g) {
    u32x4 (&V)[4] = R[g % RING];
    u32x4 P[4];
#define PB(t, LN) { const unsigned bc = rowbc<LN>(pk[g - NT]); P[t] = u32x4{bc & mk0, bc & mk1, bc & mk2, bc & mk3}; }
    PB(0, 0) PB(1, 8) PB(2, 4) PB(3, 12)
#undef PB
    PIN4(P);
    PIN4(V);
#pragma unroll
    for (int t = 0; t < 4; ++t) acc = mfma(V[t], P[t], acc);
    asm volatile("" : "+v"(acc));
    SB();
    if (g + 1 < 2 * NT && (g + RING < 2 * NT || g == NT)) refill(g);
    if (g == NT) {
      L = l_l[lane & 7];
      L += dpp<XOR1>(L); L += dpp<XOR2>(L); L += dpp<XOR7>(L);
      if (w == 0) {
        __builtin_amdgcn_raw_buffer_store_b128(u32x4{__float_as_uint(M), tag, __float_as_uint(L), tag}, rw, lane ? OOB : 0, WS_ML + (b * 64 + prod) * 128, SC1);
        asm volatile("s_nop 1");
      }
      SB();
    }
    if (g == MLSTEP) { pml = __builtin_amdgcn_raw_buffer_load_b128(rw, 0, WS_ML + (b * 64 + (prod ^ NH)) * 128, SC1); SB(); }
    if (g == 2 * NT - 4) {
#pragma unroll
      for (int ks = 0; ks < 4; ++ks) {
#pragma unroll
        for (int t = 0; t < 4; ++t) {
          const u32x2 lo = wov[8 * ks + 2 * t], hi = wov[8 * ks + 2 * t + 1];
          wb[ks][0][t] = __builtin_amdgcn_perm(hi[0], lo[0], 0x05040100u);
          wb[ks][1][t] = __builtin_amdgcn_perm(hi[0], lo[0], 0x07060302u);
          wb[ks][2][t] = __builtin_amdgcn_perm(hi[1], lo[1], 0x05040100u);
          wb[ks][3][t] = __builtin_amdgcn_perm(hi[1], lo[1], 0x07060302u);
        }
        PIN4(wb[ks]);
      }
      yv = r < 2 ? (c >> 1) * 4096 + (2 * (c & 1) + r) * 16 : OOB;
      ys = WS_Y + ((b * 64 + 8 * w) * 64 + prod) * 64;
      la_w = w * LSTR + 4 * (8 * r + c); la_r = 2 * lane;
      oo = (lane & 0x1c) ? OOB : (b * DM + 8 * prod + 2 * (lane & 3) + (lane >> 5)) * 2;
      eo = (prod == 0 && lane == 0) ? WS_EP + 256 * b : OOB;
      yb = WS_Y + (b * 64 + prod) * 4096;
      share(pml);
      asm volatile("" : "+v"(yv), "+v"(la_w), "+v"(la_r), "+v"(f), "+v"(oo), "+v"(eo));
      asm volatile("" : "+s"(ys), "+s"(yb), "+s"(out));
      SB();
    }
  }
  if (c < 8) *reinterpret_cast<f32x4*>(l_a + la_w) = acc;
  if (!have) {
#pragma nounroll
    for (int spin = 0; spin < (1 << 18) && !have; ++spin) {
      int z = 0;
      asm volatile("" : "+v"(z) :: "memory");
      share(__builtin_amdgcn_raw_buffer_load_b128(rw, z, WS_ML + (b * 64 + (prod ^ NH)) * 128, SC1));
    }
    if (!have) f = __builtin_nanf("");
  }
  __syncthreads();
  {
    f32x2 a[NW];
#pragma unroll
    for (int j = 0; j < NW; ++j) a[j] = *reinterpret_cast<const f32x2*>(l_a + j * LSTR + la_r);
    const f32x2 o = (((a[0] + a[1]) + (a[2] + a[3])) + ((a[4] + a[5]) + (a[6] + a[7]))) * f;
    const unsigned opk = cvtpk(o[0], o[1]);
    f32x4 y[4];
#define OP(ks) { const u32x4 A = {rowbc<4 * ks>(opk), rowbc<4 * ks + 1>(opk), rowbc<4 * ks + 2>(opk), rowbc<4 * ks + 3>(opk)}; \
      _Pragma("unroll") for (int e = 0; e < 4; ++e) y[e] = mfma(A, wb[ks][e], ks ? y[e] : f32x4{0.f, 0.f, 0.f, 0.f}); }
    OP(0) OP(1) OP(2) OP(3)
#undef OP
    const float y0 = r ? y[2][0] : y[0][0], y1 = r ? y[3][0] : y[1][0];
    __builtin_amdgcn_raw_buffer_store_b128(u32x4{__float_as_uint(y0), tag, __float_as_uint(y1), tag}, rw, yv, ys, SC1);
    asm volatile("s_nop 1");
  }
  if (w == 0) {
    unsigned long long ok0, ok1, ok2, ok3, ta; unsigned cnt;
    asm volatile(
      "s_mov_b64 %[ok0], 0\n\t"
      "s_mov_b64 %[ok1], 0\n\t"
      "s_mov_b64 %[ok2], 0\n\t"
      "s_mov_b64 %[ok3], 0\n\t"
      "s_mov_b32 %[cnt], 0x40000\n\t"
      "v_mov_b32 v48, %[l16]\n\t"
      "v_mov_b32 v49, %[l16]\n\t"
      "v_mov_b32 v50, %[l16]\n\t"
      "v_mov_b32 v51, %[l16]\n\t"
      ".Lg_loop%=:\n\t"
      "buffer_load_dwordx4 v[32:35], v48, %[rw], %[yb] offen offset:0 sc1\n\t"
      "buffer_load_dwordx4 v[36:39], v49, %[rw], %[yb] offen offset:1024 sc1\n\t"
      "buffer_load_dwordx4 v[40:43], v50, %[rw], %[yb] offen offset:2048 sc1\n\t"
      "buffer_load_dwordx4 v[44:47], v51, %[rw], %[yb] offen offset:3072 sc1\n\t"
      "s_waitcnt vmcnt(3)\n\t"
      "v_cmp_eq_u32_e32 vcc, %[tag], v33\n\t"
      "v_cmp_eq_u32_e64 %[ta], %[tag], v35\n\t"
      "s_andn2_b64 vcc, vcc, %[ok0]\n\t"
      "s_and_b64 vcc, vcc, %[ta]\n\t"
      "v_cndmask_b32_e32 v52, v52, v32, vcc\n\t"
      "v_cndmask_b32_e32 v56, v56, v34, vcc\n\t"
      "v_cndmask_b32_e64 v48, v48, 2.0, vcc\n\t"
      "s_or_b64 %[ok0], %[ok0], vcc\n\t"
      "s_waitcnt vmcnt(2)\n\t"
      "v_cmp_eq_u32_e32 vcc, %[tag], v37\n\t"
      "v_cmp_eq_u32_e64 %[ta], %[tag], v39\n\t"
      "s_andn2_b64 vcc, vcc, %[ok1]\n\t"
      "s_and_b64 vcc, vcc, %[ta]\n\t"
      "v_cndmask_b32_e32 v53, v53, v36, vcc\n\t"
      "v_cndmask_b32_e32 v57, v57, v38, vcc\n\t"
      "v_cndmask_b32_e64 v49, v49, 2.0, vcc\n\t"
      "s_or_b64 %[ok1], %[ok1], vcc\n\t"
      "s_waitcnt vmcnt(1)\n\t"
      "v_cmp_eq_u32_e32 vcc, %[tag], v41\n\t"
      "v_cmp_eq_u32_e64 %[ta], %[tag], v43\n\t"
      "s_andn2_b64 vcc, vcc, %[ok2]\n\t"
      "s_and_b64 vcc, vcc, %[ta]\n\t"
      "v_cndmask_b32_e32 v54, v54, v40, vcc\n\t"
      "v_cndmask_b32_e32 v58, v58, v42, vcc\n\t"
      "v_cndmask_b32_e64 v50, v50, 2.0, vcc\n\t"
      "s_or_b64 %[ok2], %[ok2], vcc\n\t"
      "s_waitcnt vmcnt(0)\n\t"
      "v_cmp_eq_u32_e32 vcc, %[tag], v45\n\t"
      "v_cmp_eq_u32_e64 %[ta], %[tag], v47\n\t"
      "s_andn2_b64 vcc, vcc, %[ok3]\n\t"
      "s_and_b64 vcc, vcc, %[ta]\n\t"
      "v_cndmask_b32_e32 v55, v55, v44, vcc\n\t"
      "v_cndmask_b32_e32 v59, v59, v46, vcc\n\t"
      "v_cndmask_b32_e64 v51, v51, 2.0, vcc\n\t"
      "s_or_b64 %[ok3], %[ok3], vcc\n\t"
      "s_and_b64 %[ta], %[ok0], %[ok1]\n\t"
      "s_and_b64 vcc, %[ok2], %[ok3]\n\t"
      "s_and_b64 %[ta], %[ta], vcc\n\t"
      "s_cmp_eq_u64 %[ta], -1\n\t"
      "s_cbranch_scc1 .Lg_done%=\n\t"
      "s_sub_u32 %[cnt], %[cnt], 1\n\t"
      "s_cmp_lg_u32 %[cnt], 0\n\t"
      "s_cbranch_scc1 .Lg_loop%=\n\t"
      "v_mov_b32 v52, 0x7fc00000\n\t"
      "v_mov_b32 v56, 0x7fc00000\n\t"
      "v_mov_b32 %[eo], 2.0\n\t"
      ".Lg_done%=:\n\t"
      "v_add_f32 v52, v52, v53\n\t"
      "v_add_f32 v54, v54, v55\n\t"
      "v_add_f32 v56, v56, v57\n\t"
      "v_add_f32 v58, v58, v59\n\t"
      "v_add_f32 v52, v52, v54\n\t"
      "v_add_f32 v56, v56, v58\n\t"
      "v_mov_b32 v60, %[tag]\n\t"
      "s_nop 0\n\t"
      "v_permlane32_swap_b32 v52, v56\n\t"
      "s_nop 0\n\t"
      "v_add_f32 v52, v52, v56\n\t"
      "v_mov_b32 v56, v52\n\t"
      "s_nop 1\n\t"
      "v_permlane16_swap_b32 v52, v56\n\t"
      "s_nop 0\n\t"
      "v_add_f32 v52, v52, v56\n\t"
      "s_nop 1\n\t"
      "v_add_f32_dpp v52, v52, v52 row_ror:8 row_mask:0xf bank_mask:0xf\n\t"
      "s_nop 1\n\t"
      "v_add_f32_dpp v52, v52, v52 row_ror:4 row_mask:0xf bank_mask:0xf\n\t"
      "v_cvt_pk_bf16_f32 v52, v52, v52\n\t"
      "buffer_store_short v52, %[oo], %[ro], 0 offen\n\t"
      "buffer_store_dword v60, %[eo], %[rw], 0 offen sc1\n\t"
      : [ok0] "=&s"(ok0), [ok1] "=&s"(ok1), [ok2] "=&s"(ok2), [ok3] "=&s"(ok3), [ta] "=&s"(ta), [cnt] "=&s"(cnt), [eo] "+v"(eo)
      : [l16] "v"(l16), [oo] "v"(oo), [rw] "s"(rw), [ro] "s"(rsrc(out, NB * DM * 2)), [yb] "s"(yb), [tag] "s"(tag)
      : "memory", "vcc", "scc", "v32", "v33", "v34", "v35", "v36", "v37", "v38", "v39", "v40", "v41", "v42", "v43", "v44", "v45", "v46", "v47", "v48", "v49", "v50", "v51", "v52", "v53", "v54", "v55", "v56", "v57", "v58", "v59", "v60");
  }
}

extern "C" void kernel_launch(void* const* d_in, const int* in_sizes, int n_in, void* d_out, int out_size, void* d_ws,
                              size_t ws_size, hipStream_t stream) {
  mha_decode<<<NWG, NTHR, 0, stream>>>((const u32x4*)d_in[0], (const u32x4*)d_in[1], (const u32x4*)d_in[2],
                                       (const u32x4*)d_in[3], (const u32x4*)d_in[4], (unsigned*)d_ws, (unsigned short*)d_out);
}
```
